```python
import torch

def _softmax(x, axis=-1):
    return torch.softmax(x, dim=axis)
def _gelu(x):
    return torch.nn.functional.gelu(x, approximate="tanh")
def _silu(x):
    return torch.nn.functional.silu(x)
def _sigmoid(x):
    return torch.sigmoid(x)
def _layernorm(x, axis=-1, eps=1e-5):
    m = x.mean(dim=axis, keepdim=True)
    v = x.var(dim=axis, keepdim=True, unbiased=False)
    return (x - m) / torch.sqrt(v + eps)
def _scatter_add(tgt, idx, src, axis=0):
    return tgt.clone().index_add_(axis, idx, src)
def _segment_sum(src, seg, n):
    out = torch.zeros((n,) + src.shape[1:], dtype=src.dtype)
    return out.index_add_(0, seg, src)
def _pad(x, pw):
    flat = tuple(v for pair in reversed(pw) for v in pair)
    return torch.nn.functional.pad(x, flat)

kernel_name = 'standalone__cheap_equivariant_egnn__graph'

def setup_inputs():

    _w = (lambda a: (lambda t: t.to(torch.bfloat16) if t.is_floating_point() else t)(torch.as_tensor(a)))
    N, deg, D, De = 12500, 32, 128, max(4, 1)
    E = N * deg
    return {
        "h": _w((lambda *s: torch.randn(*s, dtype=torch.bfloat16) * 0.1)(N, D)),
        "pos": _w((lambda *s: torch.randn(*s, dtype=torch.bfloat16) * 0.8)(N, 3)),
        "e_attr": _w((lambda *s: torch.randn(*s, dtype=torch.bfloat16) * 0.8)(E, De)),
        "src": _w((lambda lo, hi, *s: torch.randint(lo, hi, s))(0, N, E).to(torch.int64)),
        "dst": _w((lambda lo, hi, *s: torch.randint(lo, hi, s))(0, N, E).to(torch.int64)),
        "w_e": _w(0.1 * (lambda *s: torch.randn(*s, dtype=torch.bfloat16) * 0.8)(2 * D + 1 + De, D)),
        "w_x": _w(0.1 * (lambda *s: torch.randn(*s, dtype=torch.bfloat16) * 0.1)(D, 1)),
        "w_h": _w(0.1 * (lambda *s: torch.randn(*s, dtype=torch.bfloat16) * 0.1)(2 * D, D)),
    }

def reference(h, pos, e_attr, src, dst, w_e, w_x, w_h):
    N, D = h.shape[0], h.shape[1]
    diff = pos[dst] - pos[src]
    dist2 = torch.sum(diff * diff, axis=-1, keepdims=True)
    e_in = torch.concatenate([h[dst], h[src], dist2, e_attr], axis=-1)
    m_ij = _silu(e_in @ w_e)
    coord_scale = m_ij @ w_x
    pos_new = pos + _segment_sum(diff * coord_scale, dst, N)
    m_i = _segment_sum(m_ij, dst, N)
    h_new = h + _silu(torch.concatenate([h, m_i], axis=-1) @ w_h)
    return h_new, pos_new
```

```cpp
#include <hip/hip_runtime.h>
#include <hip/hip_bf16.h>
#include <stdint.h>

typedef unsigned short u16;
__device__ __forceinline__ float bf2f(u16 v){ return __uint_as_float(((unsigned)v)<<16); }
__device__ __forceinline__ u16 f2bf(float f){ unsigned u=__float_as_uint(f); u += 0x7fffu + ((u>>16)&1u); return (u16)(u>>16); }
__device__ __forceinline__ float rbf(float f){ return bf2f(f2bf(f)); }

__global__ void zero_k(float* p, int n){ int i=blockIdx.x*blockDim.x+threadIdx.x; if(i<n) p[i]=0.f; }

__global__ void edge_k(const u16* h,const u16* pos,const u16* ea,const int* src,const int* dst,const u16* we,const u16* wx,
                       float* mi,float* pd,int E){
  int e=blockIdx.x; int c=threadIdx.x;
  int s=src[e], d=dst[e];
  float df[3]; float d2=0.f;
  for(int k=0;k<3;k++){ df[k]=rbf(bf2f(pos[d*3+k])-bf2f(pos[s*3+k])); d2+=rbf(df[k]*df[k]); }
  d2=rbf(d2);
  float acc=0.f;
  for(int k=0;k<128;k++) acc+=bf2f(h[d*128+k])*bf2f(we[k*128+c]);
  for(int k=0;k<128;k++) acc+=bf2f(h[s*128+k])*bf2f(we[(128+k)*128+c]);
  acc+=d2*bf2f(we[256*128+c]);
  for(int k=0;k<4;k++) acc+=bf2f(ea[e*4+k])*bf2f(we[(257+k)*128+c]);
  float x=rbf(acc);
  float m=rbf(x/(1.f+__expf(-x)));
  __shared__ float red[128];
  red[c]=m*bf2f(wx[c]);
  __syncthreads();
  for(int o=64;o>0;o>>=1){ if(c<o) red[c]+=red[c+o]; __syncthreads(); }
  float cs=rbf(red[0]);
  atomicAdd(&mi[d*128+c],m);
  if(c<3) atomicAdd(&pd[d*3+c], rbf(df[c]*cs));
}
__global__ void node_k(const u16* h,const u16* pos,const u16* wh,const float* mi,const float* pd,u16* out,int N){
  int n=blockIdx.x; int c=threadIdx.x;
  float acc=0.f;
  for(int k=0;k<128;k++) acc+=bf2f(h[n*128+k])*bf2f(wh[k*128+c]);
  for(int k=0;k<128;k++) acc+=rbf(mi[n*128+k])*bf2f(wh[(128+k)*128+c]);
  float x=rbf(acc);
  float s=rbf(x/(1.f+__expf(-x)));
  out[n*128+c]=f2bf(bf2f(h[n*128+c])+s);
  if(c<3) out[(size_t)N*128+n*3+c]=f2bf(bf2f(pos[n*3+c])+rbf(pd[n*3+c]));
}

extern "C" void kernel_launch(void* const* d_in, const int* in_sizes, int n_in,
                              void* d_out, int out_size, void* d_ws, size_t ws_size,
                              hipStream_t stream) {
  const u16* h=(const u16*)d_in[0]; const u16* pos=(const u16*)d_in[1]; const u16* ea=(const u16*)d_in[2];
  const int* src=(const int*)d_in[3]; const int* dst=(const int*)d_in[4];
  const u16* we=(const u16*)d_in[5]; const u16* wx=(const u16*)d_in[6]; const u16* wh=(const u16*)d_in[7];
  int N=in_sizes[1]/3; int E=in_sizes[3];
  float* mi=(float*)d_ws; float* pd=mi+(size_t)N*128;
  int tot=N*128+N*3;
  zero_k<<<(tot+255)/256,256,0,stream>>>(mi,tot);
  edge_k<<<E,128,0,stream>>>(h,pos,ea,src,dst,we,wx,mi,pd,E);
  node_k<<<N,128,0,stream>>>(h,pos,wh,mi,pd,(u16*)d_out,N);
}
```

```cpp
#include <hip/hip_runtime.h>
#include <hip/hip_bf16.h>
#include <stdint.h>

typedef unsigned short u16;
using bf16x8 = __attribute__((ext_vector_type(8))) short;
using f32x4  = __attribute__((ext_vector_type(4))) float;
#define DI __device__ __forceinline__

DI float bf2f(u16 v){ return __uint_as_float(((unsigned)v)<<16); }
DI u16 f2bf(float f){ unsigned u=__float_as_uint(f); u += 0x7fffu + ((u>>16)&1u); return (u16)(u>>16); }
DI float rbf(float f){ return bf2f(f2bf(f)); }
DI unsigned pack2(float lo, float hi){ return (unsigned)f2bf(lo) | ((unsigned)f2bf(hi)<<16); }
#define MFMA(a,b,c) __builtin_amdgcn_mfma_f32_16x16x32_bf16((a),(b),(c),0,0,0)

constexpr int NPB_MAX = 52;
constexpr int CAP     = 1792;
constexpr int NT3     = 512;
constexpr int NW3     = NT3/64;
constexpr int NT1     = 1024;
constexpr int NW1     = NT1/64;
constexpr int GMAX    = 1024;
constexpr int PSTR    = 144;

constexpr int W1F_OFF = 0;
constexpr int W2F_OFF = 32;
constexpr int WHF_OFF = 72;
constexpr int NFRAG   = 136;

__global__ __launch_bounds__(NT1) void k1_sort(const int* __restrict__ src, const int* __restrict__ dst, const uint2* __restrict__ ea,
                                               const u16* __restrict__ we, const u16* __restrict__ wh,
                                               uint4* __restrict__ recs, int* __restrict__ offs, uint4* __restrict__ wfrag,
                                               int E, int G, int NPB, int B1, int chunk) {
  const int tid = threadIdx.x;
  if ((int)blockIdx.x >= B1) {
    int idx = (blockIdx.x - B1) * NT1 + tid;
    if (idx < NFRAG * 64) {
      int f = idx >> 6, l = idx & 63, r = l & 15, q = l >> 4;
      unsigned v[4];
      if (f < W2F_OFF) {
        int s = f >> 3, nt = f & 7;
        #pragma unroll
        for (int j = 0; j < 8; j += 2) {
          int k = 32*s + 8*q + j;
          v[j>>1] = (unsigned)we[k*128 + 16*nt + r] | ((unsigned)we[(k+1)*128 + 16*nt + r] << 16);
        }
      } else if (f < WHF_OFF) {
        int ff = f - W2F_OFF; int s = ff >> 3, nt = ff & 7;
        #pragma unroll
        for (int j = 0; j < 8; j += 2) {
          int k = 32*s + 8*q + j;
          unsigned lo = (k   < 133) ? (unsigned)we[(128+k)*128 + 16*nt + r] : 0u;
          unsigned hi = (k+1 < 133) ? (unsigned)we[(128+k+1)*128 + 16*nt + r] : 0u;
          v[j>>1] = lo | (hi << 16);
        }
      } else {
        int ff = f - WHF_OFF; int s = ff >> 3, nt = ff & 7;
        #pragma unroll
        for (int j = 0; j < 8; j += 2) {
          int k = 32*s + 8*q + j;
          v[j>>1] = (unsigned)wh[k*128 + 16*nt + r] | ((unsigned)wh[(k+1)*128 + 16*nt + r] << 16);
        }
      }
      wfrag[idx] = make_uint4(v[0], v[1], v[2], v[3]);
    }
    return;
  }
  extern __shared__ __attribute__((aligned(16))) char smem[];
  int*   hist  = (int*)smem;
  int*   tot   = hist + NW1 * G;
  int*   wsum  = tot + G;
  uint4* stage = (uint4*)(smem + (((NW1 * G + G + 16) * 4 + 15) & ~15));
  const int b = blockIdx.x, w = tid >> 6, lane = tid & 63;
  const int e0 = b * chunk; const int n = min(chunk, E - e0);
  for (int i = tid; i < NW1 * G; i += NT1) hist[i] = 0;
  __syncthreads();
  constexpr int IT = 8;
  int my_g[IT]; uint4 my_r[IT];
  #pragma unroll
  for (int it = 0; it < IT; ++it) {
    int i = it * NT1 + tid;
    my_g[it] = -1;
    if (i < n) {
      int e = e0 + i; int d = dst[e]; int s = src[e]; uint2 a = ea[e];
      my_g[it] = d / NPB; my_r[it] = make_uint4((unsigned)s, (unsigned)d, a.x, a.y);
      atomicAdd(&hist[w * G + my_g[it]], 1);
    }
  }
  __syncthreads();
  for (int g = tid; g < G; g += NT1) {
    int run = 0;
    #pragma unroll
    for (int ww = 0; ww < NW1; ++ww) { int c = hist[ww * G + g]; hist[ww * G + g] = run; run += c; }
    tot[g] = run;
  }
  __syncthreads();
  {
    int v = (tid < G) ? tot[tid] : 0;
    int x = v;
    #pragma unroll
    for (int o = 1; o < 64; o <<= 1) { int y = __shfl_up(x, o); if (lane >= o) x += y; }
    if (lane == 63) wsum[w] = x;
    __syncthreads();
    int base = 0;
    for (int ww = 0; ww < w; ++ww) base += wsum[ww];
    int excl = base + x - v;
    if (tid < G) { tot[tid] = excl; offs[b * (G + 1) + tid] = excl; }
    if (tid == 0) offs[b * (G + 1) + G] = n;
  }
  __syncthreads();
  #pragma unroll
  for (int it = 0; it < IT; ++it) {
    if (my_g[it] >= 0) {
      int g = my_g[it];
      int p = tot[g] + atomicAdd(&hist[w * G + g], 1);
      stage[p] = my_r[it];
    }
  }
  __syncthreads();
  for (int i = tid; i < n; i += NT1) recs[e0 + i] = stage[i];
}

struct __attribute__((packed, aligned(8))) NoPad {};

__global__ __launch_bounds__(NT3) void k3_main(const u16* __restrict__ h, const u16* __restrict__ pos,
                                               const uint4* __restrict__ recs, const int* __restrict__ offs,
                                               const uint4* __restrict__ wfrag, const u16* __restrict__ wx,
                                               u16* __restrict__ out, int N, int G, int NPB, int B1, int chunk) {
  extern __shared__ __attribute__((aligned(16))) char smem[];
  uint4* W2f   = (uint4*)smem;
  float* P     = (float*)(smem + 40960);
  float* accM  = (float*)(smem + 40960 + 29952);
  float* accPos= (float*)(smem + 97536);
  int*   segS  = (int*)(smem + 98560);
  int*   segB  = (int*)(smem + 98560 + 2064);
  int*   cur   = (int*)(smem + 102672);
  int*   nbeg  = (int*)(smem + 104720);
  int*   misc  = (int*)(smem + 104992);
  int*   srcS  = (int*)(smem + 105056);
  uint4* featS = (uint4*)(smem + 112224);
  u16*   vS    = (u16*)(smem + 140896);
  u16*   nodeS = (u16*)(smem + 151648);
  u16*   Amat  = (u16*)smem;

  const int tid = threadIdx.x, w = tid >> 6, lane = tid & 63, r = lane & 15, q = lane >> 4;
  const int g = blockIdx.x;
  const int n0 = g * NPB; const int nn = min(NPB, N - n0);

  {
    int cnt = 0, beg = 0;
    if (tid < B1) { beg = offs[tid * (G + 1) + g]; int end = offs[tid * (G + 1) + g + 1]; cnt = end - beg; segB[tid] = beg; }
    int x = cnt;
    #pragma unroll
    for (int o = 1; o < 64; o <<= 1) { int y = __shfl_up(x, o); if (lane >= o) x += y; }
    if (lane == 63) misc[w] = x;
    __syncthreads();
    int base = 0;
    for (int ww = 0; ww < w; ++ww) base += misc[ww];
    if (tid < B1) segS[tid] = base + x - cnt;
    if (tid == NT3 - 1) segS[B1] = base + x;
  }
  for (int i = tid; i < 40 * 64; i += NT3) W2f[i] = wfrag[W2F_OFF * 64 + i];
  for (int i = tid; i < NPB_MAX * 128; i += NT3) accM[i] = 0.f;
  if (tid < 256) accPos[tid] = 0.f;
  {
    bf16x8 bfr[4];
    #pragma unroll
    for (int s = 0; s < 4; ++s) { uint4 v = wfrag[(W1F_OFF + s * 8 + w) * 64 + lane]; bfr[s] = __builtin_bit_cast(bf16x8, v); }
    #pragma unroll
    for (int mt = 0; mt < 4; ++mt) {
      int node = n0 + 16 * mt + r; if (node >= N) node = N - 1;
      f32x4 acc = {0.f, 0.f, 0.f, 0.f};
      #pragma unroll
      for (int s = 0; s < 4; ++s) {
        uint4 v = *(const uint4*)(h + (size_t)node * 128 + 32 * s + 8 * q);
        acc = MFMA(__builtin_bit_cast(bf16x8, v), bfr[s], acc);
      }
      #pragma unroll
      for (int i = 0; i < 4; ++i) { int ln = 16 * mt + 4 * q + i; if (ln < NPB_MAX) P[ln * PSTR + 16 * w + r] = acc[i]; }
    }
  }
  float wxr[8];
  #pragma unroll
  for (int nt = 0; nt < 8; ++nt) wxr[nt] = bf2f(wx[16 * nt + r]);
  __syncthreads();
  const int T = segS[B1];

  for (int rb = 0; rb < T; rb += CAP) {
    const int Tr = min(CAP, T - rb);
    constexpr int RK = (CAP + NT3 - 1) / NT3;
    uint4 rec[RK]; int nl[RK];
    for (int i = tid; i < NW3 * 64; i += NT3) cur[i] = 0;
    __syncthreads();
    #pragma unroll
    for (int k = 0; k < RK; ++k) {
      int p = k * NT3 + tid; nl[k] = -1;
      if (p < Tr) {
        int gp = rb + p;
        int lo = 0, hi = B1;
        while (hi - lo > 1) { int mid = (lo + hi) >> 1; if (segS[mid] <= gp) lo = mid; else hi = mid; }
        rec[k] = recs[(size_t)lo * chunk + segB[lo] + (gp - segS[lo])];
        nl[k] = (int)rec[k].y - n0;
        atomicAdd(&cur[w * 64 + nl[k]], 1);
      }
    }
    __syncthreads();
    if (tid < 64) {
      int run = 0;
      int tot = 0;
      int c[NW3];
      #pragma unroll
      for (int ww = 0; ww < NW3; ++ww) { c[ww] = cur[ww * 64 + tid]; tot += c[ww]; }
      int x = tot;
      #pragma unroll
      for (int o = 1; o < 64; o <<= 1) { int y = __shfl_up(x, o); if (lane >= o) x += y; }
      run = x - tot;
      nbeg[tid] = run;
      #pragma unroll
      for (int ww = 0; ww < NW3; ++ww) { cur[ww * 64 + tid] = run; run += c[ww]; }
      if (tid == 63) nbeg[64] = run;
    }
    __syncthreads();
    #pragma unroll
    for (int k = 0; k < RK; ++k) {
      if (nl[k] >= 0) {
        int p = atomicAdd(&cur[w * 64 + nl[k]], 1);
        int s = (int)rec[k].x;
        int d = (int)rec[k].y;
        srcS[p] = s; nodeS[p] = (u16)nl[k];
        float df[3], d2 = 0.f;
        #pragma unroll
        for (int c = 0; c < 3; ++c) { df[c] = rbf(bf2f(pos[d * 3 + c]) - bf2f(pos[(size_t)s * 3 + c])); d2 += rbf(df[c] * df[c]); vS[c * CAP + p] = f2bf(df[c]); }
        featS[p] = make_uint4((unsigned)f2bf(d2) | (rec[k].z << 16), (rec[k].z >> 16) | (rec[k].w << 16), rec[k].w >> 16, 0u);
      }
    }
    {
      int Tp = (Tr + 31) & ~31;
      for (int p = Tr + tid; p < Tp; p += NT3) { srcS[p] = 0; nodeS[p] = 0xFFFF; featS[p] = make_uint4(0,0,0,0);
        vS[p] = 0; vS[CAP + p] = 0; vS[2 * CAP + p] = 0; }
    }
    __syncthreads();

    const int TP = (Tr + 31) >> 5;
    const int tp0 = (TP * w) / NW3, tp1 = (TP * (w + 1)) / NW3;
    f32x4 Y[8];
    #pragma unroll
    for (int nt = 0; nt < 8; ++nt) Y[nt] = f32x4{0.f, 0.f, 0.f, 0.f};
    int wb = -1000;
    auto flush = [&](int base) {
      int node = base + q;
      float z[3] = {0.f, 0.f, 0.f};
      #pragma unroll
      for (int nt = 0; nt < 8; ++nt) { z[0] += Y[nt][1] * wxr[nt]; z[1] += Y[nt][2] * wxr[nt]; z[2] += Y[nt][3] * wxr[nt]; }
      #pragma unroll
      for (int o = 1; o < 16; o <<= 1) { z[0] += __shfl_xor(z[0], o); z[1] += __shfl_xor(z[1], o); z[2] += __shfl_xor(z[2], o); }
      if (node >= 0 && node < nn) {
        #pragma unroll
        for (int nt = 0; nt < 8; ++nt) atomicAdd(&accM[node * 128 + 16 * nt + r], Y[nt][0]);
        if (r < 3) atomicAdd(&accPos[node * 4 + r], r == 0 ? z[0] : (r == 1 ? z[1] : z[2]));
      }
      #pragma unroll
      for (int nt = 0; nt < 8; ++nt) Y[nt] = f32x4{0.f, 0.f, 0.f, 0.f};
    };
    for (int tp = tp0; tp < tp1; ++tp) {
      const int eb = tp * 32;
      f32x4 acc[2][8];
      bf16x8 af[2][5];
      #pragma unroll
      for (int t = 0; t < 2; ++t) {
        int s = srcS[eb + 16 * t + r];
        const uint4* hp = (const uint4*)(h + (size_t)s * 128 + 8 * q);
        #pragma unroll
        for (int k = 0; k < 4; ++k) af[t][k] = __builtin_bit_cast(bf16x8, hp[4 * k]);
        uint4 f = featS[eb + 16 * t + r];
        if (q != 0) f = make_uint4(0, 0, 0, 0);
        af[t][4] = __builtin_bit_cast(bf16x8, f);
        #pragma unroll
        for (int i = 0; i < 4; ++i) {
          int nd = nodeS[eb + 16 * t + 4 * q + i]; if (nd > NPB_MAX - 1) nd = 0;
          const float* pr = P + nd * PSTR + r;
          #pragma unroll
          for (int nt = 0; nt < 8; ++nt) acc[t][nt][i] = pr[16 * nt];
        }
      }
      #pragma unroll
      for (int k = 0; k < 5; ++k) {
        #pragma unroll
        for (int nt = 0; nt < 8; ++nt) {
          bf16x8 bf_ = __builtin_bit_cast(bf16x8, W2f[(k * 8 + nt) * 64 + lane]);
          acc[0][nt] = MFMA(af[0][k], bf_, acc[0][nt]);
          acc[1][nt] = MFMA(af[1][k], bf_, acc[1][nt]);
        }
      }
      bf16x8 xb[8];
      #pragma unroll
      for (int nt = 0; nt < 8; ++nt) {
        unsigned pk[4];
        #pragma unroll
        for (int t = 0; t < 2; ++t) {
          float y[4];
          #pragma unroll
          for (int i = 0; i < 4; ++i) {
            float x = rbf(acc[t][nt][i]);
            y[i] = x * __builtin_amdgcn_rcpf(1.f + __builtin_amdgcn_exp2f(x * -1.4426950408889634f));
          }
          pk[2 * t]     = pack2(y[0], y[1]);
          pk[2 * t + 1] = pack2(y[2], y[3]);
        }
        xb[nt] = __builtin_bit_cast(bf16x8, make_uint4(pk[0], pk[1], pk[2], pk[3]));
      }
      int nfirst = nodeS[eb];
      int last = min(eb + 31, Tr - 1);
      int nlast = nodeS[last];
      int wstart;
      if (nlast >= wb + 4) { if (wb >= 0) flush(wb); wb = nfirst; }
      wstart = wb;
      int c = r & 3, slot = r >> 2;
      u16 nd8[8]; u16 vv[8];
      #pragma unroll
      for (int t = 0; t < 2; ++t)
        #pragma unroll
        for (int i = 0; i < 4; ++i) {
          int e = eb + 16 * t + 4 * q + i;
          nd8[4 * t + i] = nodeS[e];
          vv[4 * t + i] = (c == 0) ? (u16)0x3F80 : vS[(c - 1) * CAP + e];
        }
      while (true) {
        bf16x8 sfr;
        #pragma unroll
        for (int j = 0; j < 8; ++j) sfr[j] = ((int)nd8[j] == wstart + slot) ? (short)vv[j] : (short)0;
        #pragma unroll
        for (int nt = 0; nt < 8; ++nt) Y[nt] = MFMA(sfr, xb[nt], Y[nt]);
        if (nlast < wstart + 4) break;
        flush(wstart);
        wstart += 4; wb = wstart;
      }
    }
    if (wb >= 0) flush(wb);
    __syncthreads();
  }

  for (int i = tid; i < 64 * 16; i += NT3) {
    int row = i >> 4, ch = i & 15;
    int node = n0 + row; if (node >= N) node = N - 1;
    *(uint4*)(Amat + row * 264 + 8 * ch) = *(const uint4*)(h + (size_t)node * 128 + 8 * ch);
  }
  for (int i = tid; i < 64 * 64; i += NT3) {
    int row = i >> 6, c2 = i & 63;
    unsigned v = 0;
    if (row < NPB_MAX) v = pack2(accM[row * 128 + 2 * c2], accM[row * 128 + 2 * c2 + 1]);
    *(unsigned*)(Amat + row * 264 + 128 + 2 * c2) = v;
  }
  __syncthreads();
  {
    bf16x8 bfr[8];
    #pragma unroll
    for (int s = 0; s < 8; ++s) { uint4 v = wfrag[(WHF_OFF + s * 8 + w) * 64 + lane]; bfr[s] = __builtin_bit_cast(bf16x8, v); }
    #pragma unroll
    for (int mt = 0; mt < 4; ++mt) {
      if (16 * mt >= nn) break;
      f32x4 acc = {0.f, 0.f, 0.f, 0.f};
      #pragma unroll
      for (int s = 0; s < 8; ++s) {
        bf16x8 a = *(const bf16x8*)(Amat + (16 * mt + r) * 264 + 32 * s + 8 * q);
        acc = MFMA(a, bfr[s], acc);
      }
      #pragma unroll
      for (int i = 0; i < 4; ++i) {
        int ln = 16 * mt + 4 * q + i;
        if (ln < nn) {
          float x = rbf(acc[i]);
          float y = rbf(x * __builtin_amdgcn_rcpf(1.f + __builtin_amdgcn_exp2f(x * -1.4426950408889634f)));
          float hv = bf2f(Amat[ln * 264 + 16 * w + r]);
          out[(size_t)(n0 + ln) * 128 + 16 * w + r] = f2bf(hv + y);
        }
      }
    }
  }
  for (int i = tid; i < nn * 3; i += NT3) {
    int ln = i / 3, c = i - 3 * ln;
    out[(size_t)N * 128 + (size_t)(n0 + ln) * 3 + c] = f2bf(bf2f(pos[(size_t)(n0 + ln) * 3 + c]) + rbf(accPos[ln * 4 + c]));
  }
}

extern "C" void kernel_launch(void* const* d_in, const int* in_sizes, int n_in,
                              void* d_out, int out_size, void* d_ws, size_t ws_size,
                              hipStream_t stream) {
  const u16* h=(const u16*)d_in[0]; const u16* pos=(const u16*)d_in[1]; const uint2* ea=(const uint2*)d_in[2];
  const int* src=(const int*)d_in[3]; const int* dst=(const int*)d_in[4];
  const u16* we=(const u16*)d_in[5]; const u16* wx=(const u16*)d_in[6]; const u16* wh=(const u16*)d_in[7];
  int N=in_sizes[1]/3; int E=in_sizes[3];
  int NPB = (N + 255) / 256; if (NPB > 49) NPB = 49; if (NPB < 1) NPB = 1;
  int G = (N + NPB - 1) / NPB;
  int B1 = 128; int chunk = (E + B1 - 1) / B1;
  char* ws = (char*)d_ws;
  uint4* recs = (uint4*)ws;                       size_t o = (size_t)B1 * chunk * 16;
  int* offs = (int*)(ws + o);                     o += (size_t)B1 * (G + 1) * 4; o = (o + 255) & ~(size_t)255;
  uint4* wfrag = (uint4*)(ws + o);
  int nprep = (NFRAG * 64 + NT1 - 1) / NT1;
  size_t sm1 = (((NW1 * G + G + 16) * 4 + 15) & ~15) + (size_t)chunk * 16;
  k1_sort<<<B1 + nprep, NT1, sm1, stream>>>(src, dst, ea, we, wh, recs, offs, wfrag, E, G, NPB, B1, chunk);
  k3_main<<<G, NT3, 155232, stream>>>(h, pos, recs, offs, wfrag, wx, (u16*)d_out, N, G, NPB, B1, chunk);
}
```

```cpp
#include <hip/hip_runtime.h>
#include <hip/hip_bf16.h>
#include <stdint.h>

typedef unsigned short u16;
using bf16x8 = __attribute__((ext_vector_type(8))) short;
using bf16x4 = __attribute__((ext_vector_type(4))) short;
using f32x4  = __attribute__((ext_vector_type(4))) float;
#define DI __device__ __forceinline__

DI float bf2f(u16 v){ return __uint_as_float(((unsigned)v)<<16); }
DI unsigned cvtpk(float lo, float hi){ unsigned r; asm("v_cvt_pk_bf16_f32 %0, %1, %2" : "=v"(r) : "v"(lo), "v"(hi)); return r; }
DI u16 f2bf(float f){ return (u16)(cvtpk(f, f) & 0xffffu); }
DI float rbf(float f){ return __uint_as_float(cvtpk(f, f) << 16); }
DI float lo2f(unsigned p){ return __uint_as_float(p << 16); }
DI float hi2f(unsigned p){ return __uint_as_float(p & 0xffff0000u); }
DI float silu_f(float x){ return x * __builtin_amdgcn_rcpf(1.f + __builtin_amdgcn_exp2f(x * -1.4426950408889634f)); }
#define MFMA(a,b,c) __builtin_amdgcn_mfma_f32_16x16x32_bf16((a),(b),(c),0,0,0)
#define MFMA16(a,b,c) __builtin_amdgcn_mfma_f32_16x16x16bf16_1k((a),(b),(c),0,0,0)

#define STAMP(i)

constexpr int NPB_MAX = 52;
constexpr int CAP     = 1792;
constexpr int NT3     = 768;
constexpr int NW3     = NT3/64;
constexpr int NT1     = 1024;
constexpr int NW1     = NT1/64;
constexpr int PSTR    = 144;

constexpr int W1F_OFF = 0;
constexpr int W2F_OFF = 32;
constexpr int WHF_OFF = 72;
constexpr int NFRAG   = 136;

__global__ __launch_bounds__(NT1) void k1_sort(const int* __restrict__ src, const int* __restrict__ dst, const uint2* __restrict__ ea,
                                               const u16* __restrict__ pos,
                                               const u16* __restrict__ we, const u16* __restrict__ wh,
                                               uint4* __restrict__ recA, uint2* __restrict__ recB, int* __restrict__ offs, uint4* __restrict__ wfrag,
                                               int E, int G, int NPB, int B1, int chunk) {
  const int tid = threadIdx.x;
  if ((int)blockIdx.x >= B1) {
    int idx = (blockIdx.x - B1) * NT1 + tid;
    if (idx < NFRAG * 64) {
      int f = idx >> 6, l = idx & 63, r = l & 15, q = l >> 4;
      unsigned v[4];
      if (f < W2F_OFF) {
        int s = f >> 3, nt = f & 7;
        #pragma unroll
        for (int j = 0; j < 8; j += 2) {
          int k = 32*s + 8*q + j;
          v[j>>1] = (unsigned)we[k*128 + 16*nt + r] | ((unsigned)we[(k+1)*128 + 16*nt + r] << 16);
        }
      } else if (f < WHF_OFF) {
        int ff = f - W2F_OFF; int s = ff >> 3, nt = ff & 7;
        #pragma unroll
        for (int j = 0; j < 8; j += 2) {
          int k = 32*s + 8*q + j;
          unsigned lo = (k   < 133) ? (unsigned)we[(128+k)*128 + 16*nt + r] : 0u;
          unsigned hi = (k+1 < 133) ? (unsigned)we[(128+k+1)*128 + 16*nt + r] : 0u;
          v[j>>1] = lo | (hi << 16);
        }
      } else {
        int ff = f - WHF_OFF; int s = ff >> 3, nt = ff & 7;
        #pragma unroll
        for (int j = 0; j < 8; j += 2) {
          int k = 32*s + 8*q + j;
          v[j>>1] = (unsigned)wh[k*128 + 16*nt + r] | ((unsigned)wh[(k+1)*128 + 16*nt + r] << 16);
        }
      }
      wfrag[idx] = make_uint4(v[0], v[1], v[2], v[3]);
    }
    return;
  }
  extern __shared__ __attribute__((aligned(16))) char smem[];
  int*   hist  = (int*)smem;
  int*   tot   = hist + NW1 * G;
  int*   wsum  = tot + G;
  const int stoff = (((NW1 * G + G + 16) * 4 + 15) & ~15);
  uint4* stageA = (uint4*)(smem + stoff);
  uint2* stageB = (uint2*)(smem + stoff + chunk * 16);
  const int b = blockIdx.x, w = tid >> 6, lane = tid & 63;
  const int e0 = b * chunk; const int n = min(chunk, E - e0);
  for (int i = tid; i < NW1 * G; i += NT1) hist[i] = 0;
  constexpr int IT = 4;
  int my_s[IT], my_d[IT]; uint2 my_a[IT];
  #pragma unroll
  for (int it = 0; it < IT; ++it) {
    int i = it * NT1 + tid; int e = e0 + min(i, n - 1);
    my_d[it] = dst[e]; my_s[it] = src[e]; my_a[it] = ea[e];
  }
  u16 ps[IT][3], pd[IT][3];
  #pragma unroll
  for (int it = 0; it < IT; ++it) {
    #pragma unroll
    for (int c = 0; c < 3; ++c) { ps[it][c] = pos[(size_t)my_s[it] * 3 + c]; pd[it][c] = pos[(size_t)my_d[it] * 3 + c]; }
  }
  __syncthreads();
  int my_g[IT];
  #pragma unroll
  for (int it = 0; it < IT; ++it) {
    int i = it * NT1 + tid;
    my_g[it] = my_d[it] / NPB;
    if (i < n) atomicAdd(&hist[w * G + my_g[it]], 1);
  }
  __syncthreads();
  for (int g = tid; g < G; g += NT1) {
    int run = 0;
    #pragma unroll
    for (int ww = 0; ww < NW1; ++ww) { int c = hist[ww * G + g]; hist[ww * G + g] = run; run += c; }
    tot[g] = run;
  }
  __syncthreads();
  {
    int v = (tid < G) ? tot[tid] : 0;
    int x = v;
    #pragma unroll
    for (int o = 1; o < 64; o <<= 1) { int y = __shfl_up(x, o); if (lane >= o) x += y; }
    if (lane == 63) wsum[w] = x;
    __syncthreads();
    int base = 0;
    for (int ww = 0; ww < w; ++ww) base += wsum[ww];
    int excl = base + x - v;
    if (tid < G) { tot[tid] = excl; offs[b * (G + 1) + tid] = excl; }
    if (tid == 0) offs[b * (G + 1) + G] = n;
  }
  __syncthreads();
  #pragma unroll
  for (int it = 0; it < IT; ++it) {
    int i = it * NT1 + tid;
    if (i < n) {
      int g = my_g[it];
      int p = tot[g] + atomicAdd(&hist[w * G + g], 1);
      float df[3], d2 = 0.f;
      #pragma unroll
      for (int c = 0; c < 3; ++c) { df[c] = rbf(bf2f(pd[it][c]) - bf2f(ps[it][c])); d2 += rbf(df[c] * df[c]); }
      unsigned nloc = (unsigned)(my_d[it] - g * NPB);
      stageA[p] = make_uint4((unsigned)my_s[it], (unsigned)f2bf(d2) | (my_a[it].x << 16), (my_a[it].x >> 16) | (my_a[it].y << 16), (my_a[it].y >> 16) | (nloc << 16));
      stageB[p] = make_uint2(cvtpk(df[0], df[1]), cvtpk(df[2], 0.f));
    }
  }
  __syncthreads();
  for (int i = tid; i < n; i += NT1) { recA[e0 + i] = stageA[i]; recB[e0 + i] = stageB[i]; }
}

__global__ __launch_bounds__(NT3) void k3_main(const u16* __restrict__ h, const u16* __restrict__ pos,
                                               const uint4* __restrict__ recA, const uint2* __restrict__ recB, const int* __restrict__ offs,
                                               const uint4* __restrict__ wfrag, const u16* __restrict__ wx,
                                               u16* __restrict__ out, int N, int G, int NPB, int B1, int chunk) {
  extern __shared__ __attribute__((aligned(16))) char smem[];
  uint4* W2f   = (uint4*)smem;
  float* P     = (float*)(smem + 40960);
  float* accM  = (float*)(smem + 70912);
  float* accPos= (float*)(smem + 97536);
  int*   segS  = (int*)(smem + 98560);
  int*   segB  = (int*)(smem + 100624);
  int*   cur   = (int*)(smem + 102672);
  int*   misc  = (int*)(smem + 106768);
  float* wxS   = (float*)(smem + 157008);
  int*   srcS  = (int*)(smem + 106832);
  uint4* featS = (uint4*)(smem + 114000);
  u16*   vS    = (u16*)(smem + 142672);
  u16*   nodeS = (u16*)(smem + 153424);
  u16*   Amat  = (u16*)smem;

  const int tid = threadIdx.x, w = tid >> 6, lane = tid & 63, r = lane & 15, q = lane >> 4;
  const int g = blockIdx.x;
  const int n0 = g * NPB; const int nn = min(NPB, N - n0);
  const int wnt = w & 7, wmt = (w < 8) ? 0 : 2;
  STAMP(0);
  int sbeg = 0, send = 0;
  if (tid < B1) { sbeg = offs[tid * (G + 1) + g]; send = offs[tid * (G + 1) + g + 1]; }
  if (tid < 128) wxS[tid] = bf2f(wx[tid]);
  {
    int cnt = send - sbeg;
    if (tid < B1) segB[tid] = sbeg;
    int x = cnt;
    #pragma unroll
    for (int o = 1; o < 64; o <<= 1) { int y = __shfl_up(x, o); if (lane >= o) x += y; }
    if (lane == 63) misc[w] = x;
    for (int i = tid; i < NPB_MAX * 128; i += NT3) accM[i] = 0.f;
    if (tid < 256) accPos[tid] = 0.f;
    __syncthreads();
    int base = 0;
    for (int ww = 0; ww < w; ++ww) base += misc[ww];
    if (tid < B1) segS[tid] = base + x - cnt;
    if (tid == NT3 - 1) segS[B1] = base + x;
  }
  for (int job = w; job < 32; job += NW3) {
    int mt = job >> 3, nt = job & 7;
    int node = n0 + 16 * mt + r; if (node >= N) node = N - 1;
    f32x4 acc = {0.f, 0.f, 0.f, 0.f};
    #pragma unroll
    for (int k = 0; k < 4; ++k)
      acc = MFMA(__builtin_bit_cast(bf16x8, *(const uint4*)(h + (size_t)node * 128 + 32 * k + 8 * q)),
                 __builtin_bit_cast(bf16x8, wfrag[(W1F_OFF + k * 8 + nt) * 64 + lane]), acc);
    #pragma unroll
    for (int i = 0; i < 4; ++i) { int ln = 16 * mt + 4 * q + i; if (ln < NPB_MAX) P[ln * PSTR + 16 * nt + r] = acc[i]; }
  }
  __syncthreads();
  STAMP(1);
  const int T = segS[B1];

  for (int rb = 0; rb < T; rb += CAP) {
    const int Tr = min(CAP, T - rb);
    int tq = tid; asm volatile("" : "+v"(tq));
    constexpr int RK = (CAP + NT3 - 1) / NT3;
    uint4 ra[RK]; uint2 rbv[RK]; int nl[RK];
    #pragma unroll
    for (int k = 0; k < RK; ++k) {
      int p = k * NT3 + tq; nl[k] = -1;
      {
        int gp = rb + min(p, Tr - 1);
        int lo = 0, hi = B1;
        while (hi - lo > 1) { int mid = (lo + hi) >> 1; if (segS[mid] <= gp) lo = mid; else hi = mid; }
        size_t idx = (size_t)lo * chunk + segB[lo] + (gp - segS[lo]);
        ra[k] = recA[idx]; rbv[k] = recB[idx];
      }
    }
    for (int i = tq; i < NW3 * 64; i += NT3) cur[i] = 0;
    __syncthreads();
    #pragma unroll
    for (int k = 0; k < RK; ++k) {
      int p = k * NT3 + tq;
      if (p < Tr) { nl[k] = (int)(ra[k].w >> 16); atomicAdd(&cur[w * 64 + nl[k]], 1); }
    }
    __syncthreads();
    if (tq < 64) {
      int tot = 0; int c[NW3];
      #pragma unroll
      for (int ww = 0; ww < NW3; ++ww) { c[ww] = cur[ww * 64 + tq]; tot += c[ww]; }
      int x = tot;
      #pragma unroll
      for (int o = 1; o < 64; o <<= 1) { int y = __builtin_amdgcn_ds_bpermute((tq - o) * 4, x); if (tq >= o) x += y; }
      int run = x - tot;
      #pragma unroll
      for (int ww = 0; ww < NW3; ++ww) { cur[ww * 64 + tq] = run; run += c[ww]; }
    }
    __syncthreads();
    #pragma unroll
    for (int k = 0; k < RK; ++k) {
      if (nl[k] >= 0) {
        int p = atomicAdd(&cur[w * 64 + nl[k]], 1);
        srcS[p] = (int)ra[k].x; nodeS[p] = (u16)nl[k];
        featS[p] = make_uint4(ra[k].y, ra[k].z, ra[k].w & 0xffffu, 0u);
        vS[p] = (u16)(rbv[k].x & 0xffffu); vS[CAP + p] = (u16)(rbv[k].x >> 16); vS[2 * CAP + p] = (u16)(rbv[k].y & 0xffffu);
      }
    }
    {
      int Tp = (Tr + 15) & ~15;
      for (int p = Tr + tq; p < Tp; p += NT3) { srcS[p] = 0; nodeS[p] = 0xFFFF; featS[p] = make_uint4(0,0,0,0);
        vS[p] = 0; vS[CAP + p] = 0; vS[2 * CAP + p] = 0; }
    }
    __syncthreads();
    STAMP(2);
    int tm = tid; asm volatile("" : "+v"(tm));
    const int lane = tm & 63, r = lane & 15, q = lane >> 4, w = tm >> 6;
    const int TT = (Tr + 15) >> 4;
    const int hf = w & 1, wg = w >> 1;
    const int t0 = (TT * wg) / (NW3 / 2), t1 = (TT * (wg + 1)) / (NW3 / 2);
    bf16x8 wr[5][4];
    #pragma unroll
    for (int k = 0; k < 5; ++k)
      #pragma unroll
      for (int n4 = 0; n4 < 4; ++n4) wr[k][n4] = __builtin_bit_cast(bf16x8, wfrag[(W2F_OFF + k * 8 + 4 * hf + n4) * 64 + lane]);
    f32x4 Y[4];
    #pragma unroll
    for (int nt = 0; nt < 4; ++nt) Y[nt] = f32x4{0.f, 0.f, 0.f, 0.f};
    int wb = -1000;
    auto flush = [&](int base) {
      int node = base + q;
      float z[3] = {0.f, 0.f, 0.f};
      #pragma unroll
      for (int nt = 0; nt < 4; ++nt) { float wv = wxS[64 * hf + 16 * nt + r]; z[0] += Y[nt][1] * wv; z[1] += Y[nt][2] * wv; z[2] += Y[nt][3] * wv; }
      #pragma unroll
      for (int o = 1; o < 16; o <<= 1) { z[0] += __shfl_xor(z[0], o); z[1] += __shfl_xor(z[1], o); z[2] += __shfl_xor(z[2], o); }
      if (node >= 0 && node < nn) {
        #pragma unroll
        for (int nt = 0; nt < 4; ++nt) atomicAdd(&accM[node * 128 + 64 * hf + 16 * nt + r], Y[nt][0]);
        if (r < 3) atomicAdd(&accPos[node * 4 + r], r == 0 ? z[0] : (r == 1 ? z[1] : z[2]));
      }
      #pragma unroll
      for (int nt = 0; nt < 4; ++nt) Y[nt] = f32x4{0.f, 0.f, 0.f, 0.f};
    };
    const int cc = r & 3, slot = r >> 2;
    const u16* vsel = vS + (cc > 0 ? cc - 1 : 0) * CAP;
    for (int tt = t0; tt < t1; ++tt) {
      const int eb = tt * 16;
      bf16x8 af[5];
      {
        int sidx = srcS[eb + r];
        const uint4* hp = (const uint4*)(h + (size_t)sidx * 128 + 8 * q);
        #pragma unroll
        for (int k = 0; k < 4; ++k) af[k] = __builtin_bit_cast(bf16x8, hp[4 * k]);
        uint4 f = featS[eb + r];
        if (q != 0) f = make_uint4(0, 0, 0, 0);
        af[4] = __builtin_bit_cast(bf16x8, f);
      }
      uint2 ndq = *(const uint2*)(nodeS + eb + 4 * q);
      uint2 vq  = *(const uint2*)(vsel + eb + 4 * q);
      if (cc == 0) vq = make_uint2(0x3F803F80u, 0x3F803F80u);
      f32x4 acc[4];
      #pragma unroll
      for (int i = 0; i < 4; ++i) {
        unsigned nd = (i < 2 ? ndq.x : ndq.y); nd = (i & 1) ? (nd >> 16) : (nd & 0xffffu);
        if (nd > NPB_MAX - 1) nd = 0;
        const float* pr = P + nd * PSTR + 64 * hf + r;
        #pragma unroll
        for (int n4 = 0; n4 < 4; ++n4) acc[n4][i] = pr[16 * n4];
      }
      #pragma unroll
      for (int k = 0; k < 5; ++k)
        #pragma unroll
        for (int n4 = 0; n4 < 4; ++n4) acc[n4] = MFMA(af[k], wr[k][n4], acc[n4]);
      uint2 xb[4];
      #pragma unroll
      for (int n4 = 0; n4 < 4; ++n4) {
        unsigned pk[2];
        #pragma unroll
        for (int i2 = 0; i2 < 2; ++i2) {
          unsigned xr = cvtpk(acc[n4][2 * i2], acc[n4][2 * i2 + 1]);
          float y0 = silu_f(lo2f(xr)), y1 = silu_f(hi2f(xr));
          pk[i2] = cvtpk(y0, y1);
        }
        xb[n4] = make_uint2(pk[0], pk[1]);
      }
      int nfirst = nodeS[eb];
      int nlast = nodeS[min(eb + 15, Tr - 1)];
      if (nlast >= wb + 4) { if (wb >= 0) flush(wb); wb = nfirst; }
      int wstart = wb;
      while (true) {
        unsigned tgt = (unsigned)(wstart + slot);
        unsigned m0 = ((ndq.x & 0xffffu) == tgt ? 0xffffu : 0u) | ((ndq.x >> 16) == tgt ? 0xffff0000u : 0u);
        unsigned m1 = ((ndq.y & 0xffffu) == tgt ? 0xffffu : 0u) | ((ndq.y >> 16) == tgt ? 0xffff0000u : 0u);
        bf16x4 sfr = __builtin_bit_cast(bf16x4, make_uint2(vq.x & m0, vq.y & m1));
        #pragma unroll
        for (int nt = 0; nt < 4; ++nt) Y[nt] = MFMA16(sfr, __builtin_bit_cast(bf16x4, xb[nt]), Y[nt]);
        if (nlast < wstart + 4) break;
        flush(wstart);
        wstart += 4; wb = wstart;
      }
    }
    if (wb >= 0) flush(wb);
    STAMP(3);
    __syncthreads();
  }
  STAMP(4);
  {
  int te = tid; asm volatile("" : "+v"(te));
  const int we_ = te >> 6, le = te & 63, re = le & 15, qe = le >> 4;
  bf16x8 bh[8];
  for (int i = te; i < 64 * 16; i += NT3) {
    int row = i >> 4, ch = i & 15;
    int node = n0 + row; if (node >= N) node = N - 1;
    *(uint4*)(Amat + row * 264 + 8 * ch) = *(const uint4*)(h + (size_t)node * 128 + 8 * ch);
  }
  for (int i = te; i < 64 * 64; i += NT3) {
    int row = i >> 6, c2 = i & 63;
    unsigned v = 0;
    if (row < NPB_MAX) v = cvtpk(accM[row * 128 + 2 * c2], accM[row * 128 + 2 * c2 + 1]);
    *(unsigned*)(Amat + row * 264 + 128 + 2 * c2) = v;
  }
  __syncthreads();
  for (int job = we_; job < 32; job += NW3) {
    int mt = job >> 3; const int wnt2 = job & 7;
    if (16 * mt < nn) {
      #pragma unroll
      for (int s = 0; s < 8; ++s) bh[s] = __builtin_bit_cast(bf16x8, wfrag[(WHF_OFF + s * 8 + wnt2) * 64 + le]);
      f32x4 acc = {0.f, 0.f, 0.f, 0.f};
      #pragma unroll
      for (int s = 0; s < 8; ++s) {
        bf16x8 a = *(const bf16x8*)(Amat + (16 * mt + re) * 264 + 32 * s + 8 * qe);
        acc = MFMA(a, bh[s], acc);
      }
      #pragma unroll
      for (int i = 0; i < 4; ++i) {
        int ln = 16 * mt + 4 * qe + i;
        if (ln < nn) {
          float y = rbf(silu_f(rbf(acc[i])));
          float hval = bf2f(Amat[ln * 264 + 16 * wnt2 + re]);
          out[(size_t)(n0 + ln) * 128 + 16 * wnt2 + re] = f2bf(hval + y);
        }
      }
    }
  }
  for (int i = te; i < nn * 3; i += NT3) {
    int ln = i / 3, c = i - 3 * ln;
    out[(size_t)N * 128 + (size_t)(n0 + ln) * 3 + c] = f2bf(bf2f(pos[(size_t)(n0 + ln) * 3 + c]) + rbf(accPos[ln * 4 + c]));
  }
  }
  STAMP(5);
}

extern "C" void kernel_launch(void* const* d_in, const int* in_sizes, int n_in,
                              void* d_out, int out_size, void* d_ws, size_t ws_size,
                              hipStream_t stream) {
  const u16* h=(const u16*)d_in[0]; const u16* pos=(const u16*)d_in[1]; const uint2* ea=(const uint2*)d_in[2];
  const int* src=(const int*)d_in[3]; const int* dst=(const int*)d_in[4];
  const u16* we=(const u16*)d_in[5]; const u16* wx=(const u16*)d_in[6]; const u16* wh=(const u16*)d_in[7];
  int N=in_sizes[1]/3; int E=in_sizes[3];
  int NPB = (N + 255) / 256; if (NPB > 49) NPB = 49; if (NPB < 1) NPB = 1;
  int G = (N + NPB - 1) / NPB;
  int B1 = 128; int chunk = (E + B1 - 1) / B1;
  char* ws = (char*)d_ws;
  uint4* recA = (uint4*)ws;                       size_t o = (size_t)B1 * chunk * 16;
  uint2* recB = (uint2*)(ws + o);                 o += (size_t)B1 * chunk * 8; o = (o + 255) & ~(size_t)255;
  int* offs = (int*)(ws + o);                     o += (size_t)B1 * (G + 1) * 4; o = (o + 255) & ~(size_t)255;
  uint4* wfrag = (uint4*)(ws + o);
  int nprep = (NFRAG * 64 + NT1 - 1) / NT1;
  size_t sm1 = (((NW1 * G + G + 16) * 4 + 15) & ~15) + (size_t)chunk * 24;
  k1_sort<<<B1 + nprep, NT1, sm1, stream>>>(src, dst, ea, pos, we, wh, recA, recB, offs, wfrag, E, G, NPB, B1, chunk);
  k3_main<<<G, NT3, 157520, stream>>>(h, pos, recA, recB, offs, wfrag, wx, (u16*)d_out, N, G, NPB, B1, chunk);
}
```
